```python
import torch

def _softmax(x, axis=-1):
    return torch.softmax(x, dim=axis)
def _gelu(x):
    return torch.nn.functional.gelu(x, approximate="tanh")
def _silu(x):
    return torch.nn.functional.silu(x)
def _sigmoid(x):
    return torch.sigmoid(x)
def _layernorm(x, axis=-1, eps=1e-5):
    m = x.mean(dim=axis, keepdim=True)
    v = x.var(dim=axis, keepdim=True, unbiased=False)
    return (x - m) / torch.sqrt(v + eps)
def _scatter_add(tgt, idx, src, axis=0):
    return tgt.clone().index_add_(axis, idx, src)
def _segment_sum(src, seg, n):
    out = torch.zeros((n,) + src.shape[1:], dtype=src.dtype)
    return out.index_add_(0, seg, src)
def _pad(x, pw):
    flat = tuple(v for pair in reversed(pw) for v in pair)
    return torch.nn.functional.pad(x, flat)

kernel_name = 'sequential_preLN__sliding_block__glu_gated_geglu__prompt'

def setup_inputs():

    _w = (lambda a: (lambda t: t.to(torch.bfloat16) if t.is_floating_point() else t)(torch.as_tensor(a)))
    def _A_setup():
        B, S, D = 1, 2048, 640
        return {
            "x": _w((lambda *s: torch.randn(*s, dtype=torch.bfloat16) * 0.05)(B, S, D)),
            "wqkv": _w((lambda *s: torch.randn(*s, dtype=torch.bfloat16) * 0.05)(D, 3 * D)),
            "wo": _w((lambda *s: torch.randn(*s, dtype=torch.bfloat16) * 0.05)(D, D)),
        }
    _A_in = _A_setup()

    def _B_setup():
        B, S, Dm, Df = 1, 2048, 640, 1536
        return {
            "x": _w((lambda *s: torch.randn(*s, dtype=torch.bfloat16) * 0.05)(B, S, Dm)),
            "w_gate": _w((lambda *s: torch.randn(*s, dtype=torch.bfloat16) * 0.8)(Dm, Df) * (Dm ** -0.5)),
            "w_up": _w((lambda *s: torch.randn(*s, dtype=torch.bfloat16) * 0.8)(Dm, Df) * (Dm ** -0.5)),
            "w_down": _w((lambda *s: torch.randn(*s, dtype=torch.bfloat16) * 0.8)(Df, Dm) * (Df ** -0.5)),
        }
    _B_in = _B_setup()

    return {
        "A_x": _A_in["x"],
        "A_wqkv": _A_in["wqkv"],
        "A_wo": _A_in["wo"],
        "B_x": _B_in["x"],
        "B_w_gate": _B_in["w_gate"],
        "B_w_up": _B_in["w_up"],
        "B_w_down": _B_in["w_down"],
    }

def reference(A_x, A_wqkv, A_wo, B_x, B_w_gate, B_w_up, B_w_down):
    def _A_ref(x, wqkv, wo):
        B, S, D = x.shape
        H = 32; Dh = D // H; W = 256
        qkv = (lambda x, *d: x.permute(*d))((x @ wqkv).reshape(B, S, 3, H, Dh), 2, 0, 3, 1, 4)
        q, k, v = qkv[0], qkv[1], qkv[2]
        scores = torch.einsum("bhqd,bhkd->bhqk", q, k) * (Dh ** -0.5)
        idx = torch.arange(S)
        mask = (idx[:, None] - idx[None, :] >= 0) & (idx[:, None] - idx[None, :] < W)
        scores = torch.where(mask[None, None], scores, -1e9)
        p = _softmax(scores, axis=-1)
        o = torch.einsum("bhqk,bhkd->bhqd", p, v)
        return (lambda x, *d: x.permute(*d))(o, 0, 2, 1, 3).reshape(B, S, D) @ wo

    def _B_ref(x, w_gate, w_up, w_down):
        g = torch.einsum("bsd,df->bsf", x, w_gate)
        u = torch.einsum("bsd,df->bsf", x, w_up)
        h = _gelu(g) * u
        return torch.einsum("bsf,fd->bsd", h, w_down)

    x = A_x
    h = x + _A_ref(_layernorm(x, axis=-1), A_wqkv, A_wo)
    h2 = h + _B_ref(_layernorm(h, axis=-1), B_w_gate, B_w_up, B_w_down)
    return h2
```

```cpp
#include <hip/hip_runtime.h>
#include <hip/hip_bf16.h>
typedef __hip_bfloat16 bf16;
constexpr int S = 2048, D = 640, H = 32, DH = 20, W = 256, DF = 1536;
__device__ inline float b2f(bf16 v) { return __bfloat162float(v); }
__device__ inline bf16 f2b(float v) { return __float2bfloat16(v); }

__global__ void k_ln(const bf16* x, bf16* y) {
    int r = blockIdx.x, l = threadIdx.x;
    float v[10], s = 0;
    for (int i = 0; i < 10; ++i) { v[i] = b2f(x[r * D + l + 64 * i]); s += v[i]; }
    for (int m = 32; m; m >>= 1) s += __shfl_xor(s, m);
    float mean = s / D, q = 0;
    for (int i = 0; i < 10; ++i) { float d = v[i] - mean; q += d * d; }
    for (int m = 32; m; m >>= 1) q += __shfl_xor(q, m);
    float rs = rsqrtf(q / D + 1e-5f);
    for (int i = 0; i < 10; ++i) y[r * D + l + 64 * i] = f2b((v[i] - mean) * rs);
}
__global__ void k_gemm(const bf16* A, const bf16* Wt, const bf16* res, bf16* C, int M, int N, int K) {
    __shared__ float sa[16][17], sb[16][17];
    int tx = threadIdx.x, ty = threadIdx.y;
    int row = blockIdx.y * 16 + ty, col = blockIdx.x * 16 + tx;
    float acc = 0;
    for (int k0 = 0; k0 < K; k0 += 16) {
        sa[ty][tx] = b2f(A[row * K + k0 + tx]);
        sb[ty][tx] = b2f(Wt[(k0 + ty) * N + col]);
        __syncthreads();
        for (int k = 0; k < 16; ++k) acc += sa[ty][k] * sb[k][tx];
        __syncthreads();
    }
    if (res) acc = b2f(f2b(acc)) + b2f(res[row * N + col]);
    C[row * N + col] = f2b(acc);
}
__global__ void k_attn(const bf16* qkv, bf16* o) {
    int t = blockIdx.x * blockDim.x + threadIdx.x;
    int h = t / S, q = t % S;
    float qv[DH];
    for (int d = 0; d < DH; ++d) qv[d] = b2f(qkv[q * 3 * D + h * DH + d]) * 0.22360679775f;
    float m = -1e30f, l = 0, acc[DH];
    for (int d = 0; d < DH; ++d) acc[d] = 0;
    int k0 = q - (W - 1); if (k0 < 0) k0 = 0;
    for (int k = k0; k <= q; ++k) {
        float s = 0;
        for (int d = 0; d < DH; ++d) s += qv[d] * b2f(qkv[k * 3 * D + D + h * DH + d]);
        float mn = fmaxf(m, s), a = __expf(m - mn), p = __expf(s - mn);
        l = l * a + p;
        for (int d = 0; d < DH; ++d) acc[d] = acc[d] * a + p * b2f(qkv[k * 3 * D + 2 * D + h * DH + d]);
        m = mn;
    }
    for (int d = 0; d < DH; ++d) o[q * D + h * DH + d] = f2b(acc[d] / l);
}
__global__ void k_geglu(const bf16* g, const bf16* u, bf16* a, int n) {
    int i = blockIdx.x * blockDim.x + threadIdx.x;
    if (i >= n) return;
    float x = b2f(g[i]);
    float t = tanhf(0.7978845608f * (x + 0.044715f * x * x * x));
    float ge = b2f(f2b(0.5f * x * (1.f + t)));
    a[i] = f2b(ge * b2f(u[i]));
}
extern "C" void kernel_launch(void* const* d_in, const int* in_sizes, int n_in,
                              void* d_out, int out_size, void* d_ws, size_t ws_size,
                              hipStream_t stream) {
    const bf16* x = (const bf16*)d_in[0];
    const bf16* wqkv = (const bf16*)d_in[1];
    const bf16* wo = (const bf16*)d_in[2];
    const bf16* wg = (const bf16*)d_in[4];
    const bf16* wu = (const bf16*)d_in[5];
    const bf16* wd = (const bf16*)d_in[6];
    bf16* out = (bf16*)d_out;
    bf16* ws = (bf16*)d_ws;
    bf16* ln = ws; bf16* qkv = ln + S * D; bf16* o = qkv + S * 3 * D; bf16* h = o + S * D;
    bf16* g = h + S * D; bf16* u = g + S * DF; bf16* a = u + S * DF;
    dim3 b(16, 16);
    k_ln<<<S, 64, 0, stream>>>(x, ln);
    k_gemm<<<dim3(3 * D / 16, S / 16), b, 0, stream>>>(ln, wqkv, nullptr, qkv, S, 3 * D, D);
    k_attn<<<S * H / 256, 256, 0, stream>>>(qkv, o);
    k_gemm<<<dim3(D / 16, S / 16), b, 0, stream>>>(o, wo, x, h, S, D, D);
    k_ln<<<S, 64, 0, stream>>>(h, ln);
    k_gemm<<<dim3(DF / 16, S / 16), b, 0, stream>>>(ln, wg, nullptr, g, S, DF, D);
    k_gemm<<<dim3(DF / 16, S / 16), b, 0, stream>>>(ln, wu, nullptr, u, S, DF, D);
    k_geglu<<<S * DF / 256, 256, 0, stream>>>(g, u, a, S * DF);
    k_gemm<<<dim3(D / 16, S / 16), b, 0, stream>>>(a, wd, h, out, S, D, DF);
}
```

```cpp
#include <hip/hip_runtime.h>
#include <stdint.h>

#if defined(__HIP_DEVICE_COMPILE__)
#pragma clang attribute push(__attribute__((target("no-packed-fp32-ops"))), apply_to = function)
#endif
typedef __attribute__((ext_vector_type(8))) __bf16 bf16x8;
typedef __attribute__((ext_vector_type(2))) __bf16 bf16x2;
typedef __attribute__((ext_vector_type(4))) short s16x4;
typedef __attribute__((ext_vector_type(4))) float f32x4;
typedef __attribute__((ext_vector_type(2))) float f32x2;
typedef __attribute__((ext_vector_type(16))) float f32x16;
typedef unsigned v4u __attribute__((ext_vector_type(4)));
typedef unsigned v2u __attribute__((ext_vector_type(2)));
#define LAS __attribute__((address_space(3)))
#define GAS __attribute__((address_space(1)))
#define DI __device__ __forceinline__
typedef __amdgpu_buffer_rsrc_t rsrc_t;
typedef LAS char* lptr;
DI unsigned f2u(float f) { return __builtin_bit_cast(unsigned, f); }
DI float u2f(unsigned u) { return __builtin_bit_cast(float, u); }
DI float shfl(float v, int src) { return u2f(__builtin_amdgcn_ds_bpermute(src << 2, f2u(v))); }

constexpr int S = 2048, D = 640, DF = 1536;
constexpr unsigned SZ_QKV = 32u * 64u * 1280u;
constexpr unsigned OFF_EPOCH = 0, OFF_EFLAG = 4096, OFF_QFLAG = OFF_EFLAG + 65536;
constexpr unsigned OFF_KFLAG = OFF_QFLAG + 65536, OFF_AFLAG = OFF_KFLAG + 65536;
constexpr unsigned OFF_XST = OFF_AFLAG + 2048u * 256u;
constexpr unsigned OFF_Q = OFF_XST + 16384;
constexpr unsigned OFF_K = OFF_Q + SZ_QKV;
constexpr unsigned OFF_V = OFF_K + SZ_QKV;
constexpr unsigned OFF_WO = OFF_V + SZ_QKV;
constexpr unsigned OFF_WGU = OFF_WO + 40u * 80u * 256u;
constexpr unsigned OFF_WD = OFF_WGU + 192u * 80u * 256u;
constexpr unsigned OFF_O = OFF_WD + 40u * 192u * 256u;
constexpr unsigned OFF_H = OFF_O + 128u * 80u * 256u;
constexpr unsigned OFF_HST = OFF_H + 128u * 80u * 256u;
constexpr unsigned OFF_A = OFF_HST + 2048u * 16u * 8u;
constexpr unsigned OFF_CS = OFF_A + 128u * 192u * 256u;
constexpr unsigned WS_BYTES = OFF_CS + 2u * 20u * 1536u * 4u;
constexpr unsigned OOB = 0xc0000000u;

constexpr int AUX_SC1 = 16, AUX_NT = 2;
#define MFMA16(a, b, c) __builtin_amdgcn_mfma_f32_16x16x32_bf16((a), (b), (c), 0, 0, 0)
#define MFMA32(a, b, c) __builtin_amdgcn_mfma_f32_32x32x16_bf16((a), (b), (c), 0, 0, 0)
#define MFMA32H(a, b, c) __builtin_amdgcn_mfma_f32_32x32x8bf16_1k((a), (b), (c), 0, 0, 0)
#define WAITV(n) asm volatile("s_waitcnt vmcnt(" #n ")" ::: "memory")
#define BAR() asm volatile("s_waitcnt lgkmcnt(0)\n\ts_barrier" ::: "memory")
#define SBAR() __builtin_amdgcn_sched_barrier(0)

DI rsrc_t mkrs(const void* p, unsigned bytes) { return __builtin_amdgcn_make_buffer_rsrc((void*)p, 0, bytes, 0x00020000); }
DI void set_m0(unsigned v) { asm volatile("s_mov_b32 m0, %0\n\ts_nop 0" :: "s"(v) : "memory"); }
template <int AUX, int OFF> DI void dmao(rsrc_t rs, unsigned voff, unsigned soff) {
    if constexpr (AUX == 0) asm volatile("buffer_load_dwordx4 %0, %1, %2 offen offset:%3 lds" :: "v"(voff), "s"(rs), "s"(soff), "i"(OFF) : "memory");
    else if constexpr (AUX == AUX_SC1) asm volatile("buffer_load_dwordx4 %0, %1, %2 offen offset:%3 sc1 lds" :: "v"(voff), "s"(rs), "s"(soff), "i"(OFF) : "memory");
    else asm volatile("buffer_load_dwordx4 %0, %1, %2 offen offset:%3 nt lds" :: "v"(voff), "s"(rs), "s"(soff), "i"(OFF) : "memory");
}
DI void dma4(rsrc_t rs, unsigned voff, unsigned soff) { asm volatile("buffer_load_dword %0, %1, %2 offen sc1 lds" :: "v"(voff), "s"(rs), "s"(soff) : "memory"); }
DI unsigned ldsa(lptr p) { return (unsigned)(uintptr_t)p; }
#define SGB(mask, n) __builtin_amdgcn_sched_group_barrier(mask, n, 0)
#define PIN(x) asm volatile("" : "+v"(x))
template <int AUX> DI v4u ld16(rsrc_t rs, unsigned voff, unsigned soff) { return __builtin_amdgcn_raw_buffer_load_b128(rs, voff, soff, AUX); }
template <int AUX> DI v2u ld8(rsrc_t rs, unsigned voff, unsigned soff) { return __builtin_amdgcn_raw_buffer_load_b64(rs, voff, soff, AUX); }
template <int AUX> DI void st16(rsrc_t rs, v4u v, unsigned voff, unsigned soff) { __builtin_amdgcn_raw_buffer_store_b128(v, rs, voff + soff, 0, AUX); }
template <int AUX> DI void st8(rsrc_t rs, v2u v, unsigned voff, unsigned soff) { __builtin_amdgcn_raw_buffer_store_b64(v, rs, voff, soff, AUX); }
DI bf16x8 lds16(lptr p) { return *(LAS bf16x8*)p; }
DI s16x4 ldstr(lptr p) { return __builtin_amdgcn_ds_read_tr16_b64_v4i16((LAS s16x4*)p); }
DI bf16x8 cat8(s16x4 lo, s16x4 hi) { return __builtin_bit_cast(bf16x8, __builtin_shufflevector(lo, hi, 0, 1, 2, 3, 4, 5, 6, 7)); }
DI unsigned pk2(float a, float b) { bf16x2 p = {(__bf16)a, (__bf16)b}; return __builtin_bit_cast(unsigned, p); }
constexpr unsigned TAG_X = 0x5bd1e995u;
constexpr unsigned TAG_INC = 0x9e3779b9u;
constexpr unsigned SENT = 0xffffffffu;
DI unsigned pk2s(float a, float b) { unsigned u = pk2(a, b); return u < 0xfffffffeu ? u : 0xfffffffeu; }
DI unsigned umax3(unsigned a, unsigned b, unsigned c) { unsigned t = a > b ? a : b; return t > c ? t : c; }
DI unsigned umax4(unsigned m, v4u v) { return umax3(umax3(m, v[0], v[1]), v[2], v[3]); }
DI float bflo(unsigned u) { return u2f(u << 16); }
DI float bfhi(unsigned u) { return u2f(u & 0xffff0000u); }
DI bf16x8 asb(v4u v) { return __builtin_bit_cast(bf16x8, v); }
DI float xor16_add(float v) {
    auto r = __builtin_amdgcn_permlane16_swap(f2u(v), f2u(v), false, false);
    return u2f(r[0]) + u2f(r[1]);
}
DI float xor32_add(float v) {
    auto r = __builtin_amdgcn_permlane32_swap(f2u(v), f2u(v), false, false);
    return u2f(r[0]) + u2f(r[1]);
}
DI float xor32_max(float v) {
    auto r = __builtin_amdgcn_permlane32_swap(f2u(v), f2u(v), false, false);
    return __builtin_fmaxf(u2f(r[0]), u2f(r[1]));
}

constexpr int K1_SLOT = 32768, K1_STAGE = 4 * K1_SLOT, K1_LDS = K1_STAGE + 32768;

typedef unsigned v8u __attribute__((ext_vector_type(8)));
struct alignas(32) RtTab { unsigned v[8 * 256][8]; };
constexpr RtTab make_rttab() {
    RtTab T{};
    for (int w = 0; w < 8; ++w) for (int b = 0; b < 256; ++b) {
        unsigned* o = T.v[w * 256 + b];
        if (w < 4) {
            const int t = (w >> 1) * 256 + b, ks = t / 24, cb = t - ks * 24;
            for (int j = 0; j < 4; ++j) {
                const int blk = 4 * cb + j, ci = blk / 3, bl = blk - ci * 3;
                o[j] = t < 480 ? (32 * ks + 8 * j) * 3072 + cb * 128 : OOB;
                o[4 + j] = t < 480 ? OFF_WGU + (ks * 6 + (w & 1)) * 1024 + ci * (120 * 1024) + (bl < 2 ? bl * 128 : 4096 + 1) : OOB;
            }
        } else {
            const bool isd = w < 6;
            const int t = (w & 1) * 256 + b, ks = t / 10, cb = t - ks * 10, vq = ks / 5, kl = ks - vq * 5;
            const bool ok = t < (isd ? 480 : 200), tail = !isd && kl == 4;
            const unsigned nc = isd ? 192 * 256 : 80 * 256;
            const int r0 = isd ? 32 * ks : 160 * vq + (tail ? 16 : 40 * kl), s1 = isd ? 8 : (tail ? 40 : 4), s2 = isd ? 16 : (tail ? 80 : 20);
            for (int j = 0; j < 4; ++j) {
                const int blk = 4 * cb + j, cs = blk / 5, bl = blk - cs * 5;
                o[j] = ok ? (r0 + (j & 1) * s1 + (j >> 1) * s2) * 1280 + cb * 128 + (j == 0 && tail ? 1 : 0) : OOB;
                o[4 + j] = ok ? (isd ? OFF_WD : OFF_WO) + ks * 1024 + (5 * cs + (bl & 6)) * nc + (bl & 1) * 128 + (bl == 4 ? 1 : 0) : OOB;
            }
        }
    }
    return T;
}
__constant__ const RtTab rttab = make_rttab();
DI void retile_load(const char* wo, const char* wg, const char* wu, const char* wd, int w, v8u T, int lane, unsigned stg) {
    const int e = lane >> 3, c8 = (lane & 7) << 4;
    const char* base = w < 4 ? ((w & 1) ? wu : wg) : (w < 6 ? wd : wo);
    rsrc_t rs = mkrs(base, w < 6 ? D * DF * 2 : D * D * 2);
    const unsigned vo = ((w < 6 ? 4 : ((T[0] & 1) ? 20 : 8)) * (e >> 2) + (e & 3)) * (w < 4 ? 3072 : 1280) + c8;
#pragma unroll
    for (int i = 0; i < 4; ++i) { set_m0(stg + (w < 7 ? i : 0) * 1024); dmao<AUX_NT, 0>(rs, vo, T[i] & ~1u); }
}
DI void retile_read(lptr p, bf16x8 (&f)[4]) {
#pragma unroll
    for (int j = 0; j < 4; ++j) f[j] = cat8(ldstr(p + j * 32), ldstr(p + j * 32 + 512));
}
DI void retile_store(int w, v8u T, int lane, bf16x8 (&f)[4], rsrc_t rws) {
    const int g = lane >> 4, n = lane & 15;
    const unsigned nc = w < 4 ? 2048 : (w < 6 ? 192 * 256 : 80 * 256);
    const unsigned lob = lane * 16 - 1, loa = g * 256 + (n >> 3) * 64 + (n & 3) * 16 + ((n >> 2) & 1) * nc;
#pragma unroll
    for (int j = 0; j < 4; ++j) { const unsigned d = T[4 + j]; st16<AUX_SC1>(rws, __builtin_bit_cast(v4u, f[j]), ((d & 1) ? lob : loa) + d, 0); }
    if (w < 4) {
        f32x4 pcs = {};
#pragma unroll
        for (int j = 0; j < 4; ++j) { const unsigned o1 = (n >> 2) == j ? 0x3f803f80u : 0u; pcs = MFMA16(asb(v4u{o1, o1, o1, o1}), f[j], pcs); }
        const unsigned t0 = T[0], ks = t0 / 98304u, cb2 = (t0 - ks * 98304u) * 2;
        __builtin_amdgcn_raw_buffer_store_b32(f2u(pcs[0]), rws, (t0 < OOB ? OFF_CS + ((w & 1) * 20 + ks) * 6144 + cb2 : OOB) + lane * 4, 0, AUX_SC1);
    }
}

struct Marks { unsigned a[8]; };
DI void mark_store(rsrc_t rws, const Marks& M, int i) {
    if (i == 1 || i == 4) st8<AUX_SC1>(rws, v2u{SENT, SENT}, M.a[i], 0); else st16<AUX_SC1>(rws, v4u{SENT, SENT, SENT, SENT}, M.a[i], 0);
}

template <bool ISV, class F>
DI void qkv_loop(lptr lds, rsrc_t rx, rsrc_t rw, int w,
                 unsigned va0, unsigned va1, unsigned vw0, unsigned vw1, unsigned sx, unsigned sw, lptr (&at)[2][2][4], lptr (&ab)[2][2], v4u onesv, f32x4 (&acc)[4][2], f32x4& csum, F& side) {
    const unsigned m0b = ldsa(lds) + w * 4096;
    const bf16x8 ones = asb(onesv);
    bf16x8 xf[2][4], wf[2][2];
    auto rd = [&](int s, int slot, int h) {
        const int hi = slot >> 1, o = (slot & 1) * K1_SLOT;
        if (h == 0) {
#pragma unroll
            for (int nt = 0; nt < 2; ++nt) wf[s][nt] = cat8(ldstr(ab[hi][nt] + o + s * 16384), ldstr(ab[hi][nt] + o + s * 16384 + 1024));
        } else {
#pragma unroll
            for (int mt = 0; mt < 4; ++mt) xf[s][mt] = lds16(at[hi][s][mt] + o);
        }
    };
    auto mm = [&](int s, int h) {
#pragma unroll
        for (int mt = 2 * h; mt < 2 * h + 2; ++mt)
#pragma unroll
            for (int nt = 0; nt < 2; ++nt)
                acc[mt][nt] = ISV ? MFMA16(xf[s][mt], wf[s][nt], acc[mt][nt]) : MFMA16(wf[s][nt], xf[s][mt], acc[mt][nt]);
#pragma unroll
        for (int i = 0; i < 4; ++i) { SGB(0x8, 1); SGB(0x100, 1); }
        SBAR();
    };
    auto stepx = [&]() { sx += 128; if (sx >= 1280) sx -= 1280; };
    auto stepw = [&]() { sw += 64 * 3840; if (sw >= 640 * 3840) sw -= 640 * 3840; };
    rd(0, 0, 0); rd(0, 0, 1);
    SBAR();
    stepx(); set_m0(m0b + 1 * K1_SLOT); dmao<0, 0>(rx, va0, sx); dmao<0, 1024>(rx, va1, sx);
    stepx(); set_m0(m0b + 2 * K1_SLOT); dmao<0, 0>(rx, va0, sx); dmao<0, 1024>(rx, va1, sx);
    stepx(); stepw(); set_m0(m0b + 3 * K1_SLOT);
    const bool ld = w < 4;
#pragma unroll
    for (int c = 0; c < 10; ++c) {
        rd(1, c & 3, 0);
        mm(0, 0);
        if (c == 0) { dmao<0, 0>(rx, va0, sx); dmao<0, 1024>(rx, va1, sx); }
        if (c >= 1 && c < 7) { if (ld) { set_m0(m0b + ((c - 1) & 3) * K1_SLOT + 16384); dmao<0, 0>(rx, va0, sx + 64 * 1280); dmao<0, 1024>(rx, va1, sx + 64 * 1280); } else side(2 * c); }
        if (c == 7 && !ld) side(14);
        rd(1, c & 3, 1);
        mm(0, 1);
        if (c == 0) { dmao<0, 2048>(rw, vw0, sw); dmao<0, 3072>(rw, vw1, sw); }
        if (c >= 1 && c < 7 && ld) { dmao<0, 2048>(rw, vw0, sw + 32 * 3840); dmao<0, 3072>(rw, vw1, sw + 32 * 3840); }
        csum = MFMA16(wf[0][0], ones, csum); SBAR();
        if (c == 0) WAITV(6);
        else if (ld) { if (c == 1) WAITV(12); else if (c < 7) WAITV(16); else if (c == 7) WAITV(8); else if (c == 8) WAITV(0); }
        else { if (c == 1) WAITV(4); else if (c == 2) WAITV(0); }
        if (c < 9) BAR();
        if (c < 6) { stepx(); stepw(); }
        SBAR();
        if (c < 9) rd(0, (c + 1) & 3, 0);
        mm(1, 0);
        if (c < 7) { if (ld) { if (c < 6) { set_m0(m0b + (c & 3) * K1_SLOT); dmao<0, 0>(rx, va0, sx); dmao<0, 1024>(rx, va1, sx); } } else side(2 * c + 1); }
        if (c < 9) rd(0, (c + 1) & 3, 1);
        mm(1, 1);
        if (c < 6 && ld) { dmao<0, 2048>(rw, vw0, sw); dmao<0, 3072>(rw, vw1, sw); }
        csum = MFMA16(wf[1][0], ones, csum); SBAR();
    }
}

constexpr int LDS_BYTES = 163840;
constexpr int L_K = 121600, L_V = 142080, L_MISC = 162560;
constexpr int L_EP = L_MISC + 1024, L_SF = K1_STAGE + 7 * 4096 + 2560;
constexpr int L_ONES = L_MISC, L_FLG = L_MISC + 32, L_CSUM = L_MISC + 512;
constexpr int L_SA = 81920, L_SB = 122880, L_HP = L_SB + 32768;
constexpr unsigned SPIN_MAX = 1u << 16;
enum { F_ATT = 0, F_WO = 1, F_WO2 = 2, F_WG = 3, F_CS = 4, F_GU = 5, F_DN = 6  , F_Q = 8, F_ACT = 9, F_E = 10, F_WG0 = 11 };
#define PRIO(n) __builtin_amdgcn_s_setprio(n)
DI void lf_set(lptr lds, int slot, int w) {
    asm volatile("s_waitcnt lgkmcnt(0)" ::: "memory");
    *(volatile LAS unsigned*)(lds + L_FLG + slot * 32 + w * 4) = 1u;
}
DI void lf_wait4(lptr lds, int slot, int half) {
#pragma unroll 1
    for (unsigned spins = 0; spins < (1u << 22); ++spins) {
        v4u f = *(volatile LAS v4u*)(lds + L_FLG + slot * 32 + half * 16);
        if (__builtin_amdgcn_readfirstlane(f[0] & f[1] & f[2] & f[3])) break;
        __builtin_amdgcn_s_sleep(1);
    }
    asm volatile("" ::: "memory");
}
DI void lf_wait8(lptr lds, int slot) {
#pragma unroll 1
    for (unsigned spins = 0; spins < (1u << 22); ++spins) {
        v4u f = *(volatile LAS v4u*)(lds + L_FLG + slot * 32), h = *(volatile LAS v4u*)(lds + L_FLG + slot * 32 + 16);
        f &= h;
        if (__builtin_amdgcn_readfirstlane(f[0] & f[1] & f[2] & f[3])) break;
        __builtin_amdgcn_s_sleep(1);
    }
    asm volatile("" ::: "memory");
}
DI void canary_wait(rsrc_t rws, unsigned voff, unsigned& spins) {
#pragma unroll 1
    for (;;) {
        asm volatile("" ::: "memory");
        unsigned c = __builtin_amdgcn_raw_buffer_load_b32(rws, voff, 0, AUX_SC1);
        if (__builtin_amdgcn_ballot_w64(c == SENT) == 0 || ++spins > SPIN_MAX) break;
        __builtin_amdgcn_s_sleep(2);
    }
}
DI void gflag_set(rsrc_t rws, unsigned voff, unsigned tag) { st8<AUX_SC1>(rws, v2u{tag, tag ^ TAG_X}, voff, 0); }
DI v2u gflag_load(rsrc_t rws, unsigned voff) { asm volatile("" ::: "memory"); return ld8<AUX_SC1>(rws, voff, 0); }
DI bool gflag_bad(v2u c, unsigned tag) { return __builtin_amdgcn_ballot_w64(c[0] != tag || c[1] != (tag ^ TAG_X)) != 0; }
DI void gflag_wait(rsrc_t rws, unsigned voff, unsigned tag) {
#pragma unroll 1
    for (unsigned spins = 0; spins < SPIN_MAX; ++spins) {
        if (!gflag_bad(gflag_load(rws, voff), tag)) break;
        __builtin_amdgcn_s_sleep(2);
    }
    asm volatile("" ::: "memory");
}
DI bool any_sent(unsigned mx) { return __builtin_amdgcn_ballot_w64(mx == SENT) != 0; }

template <bool EDGE, class F>
DI void attention(lptr lds, int w, int lane, const f32x16& mdiag, bf16x8 qa, s16x4 qb, f32x16& o, F& side) {
    const int hh = lane >> 5, l32 = lane & 31;
    const bool vreal = l32 < 20;
    const unsigned vstep = vreal ? 1280u : 0u, vhalf = vreal ? 640u : 0u;
    lptr k0p = lds + L_K + w * 1280 + hh * 512 + l32 * 8, k1p = lds + L_K + w * 1280 + 1024 + l32 * 8;
    lptr vp = vreal ? lds + L_V + w * 1280 + (hh * 20 + l32) * 16 : lds + L_ONES;
    const int tf = EDGE ? 8 - w : 0;
    bf16x8 k0[3]; s16x4 k1[3];
    auto ldk = [&](int t) { k0[t % 3] = cat8(*(LAS s16x4*)(k0p + t * 1280), *(LAS s16x4*)(k0p + t * 1280 + 256)); k1[t % 3] = *(LAS s16x4*)(k1p + t * 1280); };
    ldk(0); ldk(1);
    SBAR();
    f32x16 sA, sB;
    const f32x16 zero = {};
    float mx = -1e30f;
    if (EDGE) {
#pragma unroll
        for (int r = 0; r < 16; ++r) { sA[r] = -1e30f; sB[r] = -1e30f; }
    }
#pragma unroll
    for (int t = 0; t < 10; ++t) {
        if (t < 9) ldk((t + 2) % 9);
        side(0, t);
        if (t >= tf) {
            f32x16& sx = (t & 1) ? sA : sB; f32x16& sy = (t & 1) ? sB : sA;
            if (t == 0) sy = -1e30f - mdiag;
            if (t < 9) sy = MFMA32(k0[t % 3], qa, t == 0 ? sy : (t == 8 ? mdiag : zero));
            SBAR();
            if (t > 0) {
#pragma unroll
                for (int r = 0; r < 8; r += 2) mx = __builtin_fmaxf(__builtin_fmaxf(mx, sx[r]), sx[r + 1]);
                PIN(mx);
            }
            SBAR();
            if (t < 9) sy = MFMA32H(k1[t % 3], qb, sy);
            SBAR();
            if (t > 0) {
#pragma unroll
                for (int r = 8; r < 16; r += 2) mx = __builtin_fmaxf(__builtin_fmaxf(mx, sx[r]), sx[r + 1]);
                PIN(mx);
            }
            SBAR();
        }
    }
    mx = xor32_max(mx);
    f32x16 negm;
#pragma unroll
    for (int r = 0; r < 16; ++r) { negm[r] = -mx; o[r] = 0.f; if (EDGE) { sB[r] = -1e30f; sA[r] = -1e30f; } else sA[r] = mdiag[r] < 0.f ? -mx : -1e30f; }
    lptr vq = vp + (tf - 1) * vstep;
    bf16x8 v0, v1;
    v4u pa, pb = {0u, 0u, 0u, 0u};
    if (EDGE) { v0 = lds16(vq); v1 = v0; }
    PIN(negm);
    SBAR();
#define EXP4(dst, j, r) do { dst[j] = pk2(__builtin_amdgcn_exp2f(sx[r]), __builtin_amdgcn_exp2f(sx[r + 1])); dst[j + 1] = pk2(__builtin_amdgcn_exp2f(sx[r + 2]), __builtin_amdgcn_exp2f(sx[r + 3])); PIN(dst); SBAR(); } while (0)
#pragma unroll
    for (int t = 0; t < 10; ++t) {
        if (EDGE && t < 7) ldk(t + 2);
        side(1, t);
        if (t >= tf) {
            f32x16& sx = (t & 1) ? sA : sB; f32x16& sy = (t & 1) ? sB : sA;
            if (t == 8) {
#pragma unroll
                for (int r = 0; r < 16; ++r) sy[r] = mdiag[r] - mx;
            }
            if (t == 0 || t == 8) sy = MFMA32(k0[t % 3], qa, sy); else if (t < 9) sy = MFMA32(k0[t % 3], qa, negm);
            SBAR();
            if (!EDGE && t < 7) ldk(t + 2);
            if (t > 0) EXP4(pa, 0, 0);
            if (t < 9) sy = MFMA32H(k1[t % 3], qb, sy);
            SBAR();
            if (t > 0) EXP4(pa, 2, 4);
            if (t > 1 || (EDGE && t > 0)) { o = MFMA32(v1, asb(pb), o); PIN(o); }
            SBAR();
            if (t > 0) { v1 = lds16(vq + vhalf); EXP4(pb, 0, 8); o = MFMA32(v0, asb(pa), o); PIN(o); SBAR(); }
            if (t < 9) { vq += vstep; v0 = lds16(vq); }
            if (t > 0) EXP4(pb, 2, 12);
            SBAR();
        }
    }
    o = MFMA32(v1, asb(pb), o);
}
__global__ void __launch_bounds__(512, 2) mega(const char* x, const char* wqkv, const char* wo, const char* wg, const char* wu, const char* wd, char* out, char* ws) {
    extern __shared__ __attribute__((aligned(16))) char smem[];
    lptr lds = (lptr)smem;
    const int b = __builtin_amdgcn_workgroup_id_x(), tid = __builtin_amdgcn_workitem_id_x(), lane = tid & 63, w = __builtin_amdgcn_readfirstlane(tid >> 6);
    const int xc = b & 7, sl = b >> 3;
    const int p = (xc >> 1) * 4 + (sl >> 3), ct = (xc & 1) * 8 + (sl & 7);
    const int P = p >> 1, ci = (xc & 1) * 16 + (sl & 15);
    const int cs = (ci >> 4) * 4 + (ci & 3), rq = (ci >> 2) & 3;
    const int hf = w >> 2, v = w & 3;
    const int g = lane >> 4, m = lane & 15, hh = lane >> 5, l32 = lane & 31;
    const unsigned l16 = lane * 16;
    rsrc_t rws = mkrs(ws, WS_BYTES);
    const v8u rtt0 = *(const v8u*)rttab.v[v * 256 + b], rtt1 = *(const v8u*)rttab.v[(v + 4) * 256 + b];
    rsrc_t rx = mkrs(x + p * (128 * 1280), 128 * 1280), rw = mkrs(wqkv + ct * 256 - 2048, D * 3 * D * 2 - ct * 256 + 2048);
    int rot = ((sl & 7) * 5 >> 2) + 3 * (sl >> 3); rot -= rot >= 10 ? 10 : 0;
    unsigned sx = rot * 128, sw = rot * (64 * 3840);
    const unsigned va0 = (16 * w + (lane >> 3)) * 1280 + (((lane & 7) ^ g) << 4), va1 = (va0 ^ 64) + 8 * 1280 - 1024;
    const unsigned vw0 = (8 * w + g) * 3840 + ((m ^ ((g | ((w & 1) << 2)) << 1)) << 4), vw1 = vw0 + 4 * 3840 - 1024;
    if (w == 7) { set_m0(ldsa(lds) + L_EP); dma4(rws, lane * 4, OFF_EPOCH); }
    auto get_tag = [&]() { return *(volatile LAS unsigned*)(lds + L_EP) + TAG_INC; };
    if (ct < 15) { set_m0(ldsa(lds) + w * 4096); dmao<0, 0>(rx, va0, sx); dmao<0, 1024>(rx, va1, sx); dmao<0, 2048>(rw, vw0, sw); dmao<0, 3072>(rw, vw1, sw); }
    SBAR();
    if (w == 5) {
        int cu = 3 * (sl >> 3) + ((sl & 4) ? 9 : 4); cu -= cu >= 10 ? 10 : 0;
        set_m0(ldsa(lds) + K1_STAGE + 7 * 4096 + 256);
        asm volatile("buffer_load_dword %0, %1, %2 offen lds" :: "v"(lane < 32 ? (32 * (sl & 3) + lane) * 1280u : OOB), "s"(rx), "s"(cu * 128) : "memory");
    }
    if (w == 7) {
        unsigned long long pc; unsigned len;
        asm volatile("s_getpc_b64 %0\n.Lmega_pc:\n\ts_mov_b32 %1, .Lmega_end-.Lmega_pc" : "=s"(pc), "=s"(len));
        const unsigned co = (lane * 32 + sl) * 128;
        if (co < len) {
            set_m0(ldsa(lds) + K1_STAGE + 7 * 4096);
            asm volatile("global_load_lds_dword %0, off" :: "v"((const GAS char*)(pc & ~127ull) + co) : "memory");
        }
    }
    lptr at[2][2][4], ab[2][2];
    {
        const unsigned abase = (4 * hf) * 4096 + m * 128 + ((g ^ (m >> 1)) << 4);
        const int qq = m >> 2, pp = lane & 3, f = qq | ((g & 1) << 2);
        const unsigned bbase = 2048 + g * 4096 + qq * 256 + (pp & 1) * 8;
#pragma unroll
        for (int hi = 0; hi < 2; ++hi) {
#pragma unroll
            for (int s = 0; s < 2; ++s)
#pragma unroll
                for (int t = 0; t < 4; ++t) { at[hi][s][t] = lds + hi * 65536 + ((abase ^ (s * 64)) + ((t ^ v) * 4096)); PIN(at[hi][s][t]); }
#pragma unroll
            for (int nt = 0; nt < 2; ++nt) { ab[hi][nt] = lds + hi * 65536 + bbase + (((2 * (2 * v + (nt ^ hf)) + (pp >> 1)) ^ (f << 1)) << 4); PIN(ab[hi][nt]); }
        }
    }
    lptr rtp = lds + K1_STAGE + v * 4096 + (8 * g + (m >> 2)) * 128 + (lane & 3) * 8;
    PIN(rtp);
    v4u onesv = {0x3f803f80u, 0x3f803f80u, 0x3f803f80u, 0x3f803f80u};
    PIN(onesv);
    const int Pa = sl >> 2, hd_ = 4 * xc + (sl & 3);
    const int gt = 8 * Pa + w;
    const int Pg = 4 * (xc >> 2) + (sl >> 3), cg = 8 * (xc & 3) + (sl & 7);
    const int ut = 8 * Pg + (w ^ 4);
    const int om = v & 1, op = v >> 1;
    const int wt = 8 * P + 4 * hf + rq;
    Marks M;
    {
        const unsigned ob = OFF_O + (2 * gt + (l32 >> 4)) * 20480 + (hd_ >> 3) * 5120;
        M.a[0] = ob + hh * 256 + m * 16 + (hd_ & 7) * 512;
        M.a[1] = hh ? OOB : ob + m * 8 + 4096 + (hd_ & 7) * 128;
        PIN(M.a[0]); PIN(M.a[1]);
    }
    unsigned ea[2][2];
    if (ct < 10) {
        const bool isq = ct < 5;
#pragma unroll
        for (int nt = 0; nt < 2; ++nt) {
            const int c4 = 32 * (isq ? ct : ct - 5) + 8 * v + 4 * (nt ^ hf) + g, hd = (c4 * 205) >> 10, d4 = c4 - hd * 5;
            const unsigned co = (isq ? OFF_Q : OFF_K) + hd * (64 * 1280) + d4 * 256 + (4 * p + 2 * hf) * 1280;
            const unsigned im = isq ? m * 8 : (m & 7) * 8 + (m & 8) * 16, ih = isq ? 128 : 64;
            ea[nt][0] = co + im + (v & 1) * ih; ea[nt][1] = co + im + ((v & 1) ^ 1) * ih;
        }
    } else {
#pragma unroll
        for (int nt = 0; nt < 2; ++nt) {
            const int cn = 128 * (ct - 10) + 32 * v + 16 * (nt ^ hf) + m, hd = (cn * 3277) >> 16, d = cn - hd * 20;
            const unsigned co = OFF_V + hd * (64 * 1280) + (g * 20 + d) * 16 + (4 * p + 2 * hf) * 1280;
            ea[nt][0] = co + (v >> 1) * 1280; ea[nt][1] = co + ((v >> 1) ^ 1) * 1280;
        }
    }
    PIN(ea[0][0]); PIN(ea[0][1]); PIN(ea[1][0]); PIN(ea[1][1]);
    SBAR();
    LAS f32x2* rst = (LAS f32x2*)(lds + K1_STAGE + 7 * 4096 + 1024);
    LAS float* cst = (LAS float*)(lds + K1_STAGE + 7 * 4096 + 2048);
    const unsigned sfo = OFF_QFLAG + ((((p & 3) * 8 + 7) << 3) + (p >> 2) * 2 + 1) * 256;
    auto side1 = [&](int i) {
        if (i == 5 && w == 6) retile_load(wo, wg, wu, wd, 6, rtt1, lane, ldsa(lds) + K1_STAGE + 6 * 4096);
        if (i == 7 || i == 8) {
            Marks Mx;
#pragma unroll
            for (int j = 0; j < 2; ++j) { Mx.a[j] = M.a[j] - (i == 7 ? 0 : 8 * 20480); mark_store(rws, Mx, j); }
        }
        if (i == 11 && w == 6) { WAITV(4); bf16x8 f[4]; retile_read(rtp + 16384, f); retile_store(6, rtt1, lane, f, rws); }
        if (i == 11 && w == 7) { set_m0(ldsa(lds) + L_SF); dma4(rws, lane * 4, sfo); }
        if (i == 13 && w == 7 && ct < 15) {
            WAITV(0);
            const unsigned tg = get_tag();
            if (gflag_bad(*(volatile LAS v2u*)(lds + L_SF), tg)) gflag_wait(rws, sfo, tg);
            set_m0(ldsa((lptr)rst)); dmao<AUX_SC1, 0>(rws, l16, OFF_XST + p * 1024);
        }
        if (i == 14) {
            retile_load(wo, wg, wu, wd, v, rtt0, lane, ldsa(lds) + K1_STAGE + v * 4096);
            if (w < 6) retile_load(wo, wg, wu, wd, v + 4, rtt1, lane, ldsa(lds) + K1_STAGE + (v + 4) * 4096);
        }
        SBAR();
    };
    if (ct == 15) {
        v4u xv[20];
#pragma unroll
        for (int i = 0; i < 20; ++i) xv[i] = ld16<0>(rx, (16 * w + (lane >> 2)) * 1280 + (lane & 3) * 16 + i * 64, 0);
        const bf16x2 one2 = __builtin_bit_cast(bf16x2, 0x3f803f80u);
        float s1 = 0.f, s2 = 0.f;
#pragma unroll
        for (int i = 0; i < 20; ++i)
#pragma unroll
            for (int e = 0; e < 4; ++e) { const unsigned uu = xv[i][e]; bf16x2 hv = __builtin_bit_cast(bf16x2, uu); s1 = __builtin_amdgcn_fdot2_f32_bf16(hv, one2, s1, false); s2 = __builtin_amdgcn_fdot2_f32_bf16(hv, hv, s2, false); }
        s1 += shfl(s1, lane ^ 1); s1 += shfl(s1, lane ^ 2); s2 += shfl(s2, lane ^ 1); s2 += shfl(s2, lane ^ 2);
        float mean = s1 * (1.f / D), var = __builtin_fmaxf(s2 * (1.f / D) - mean * mean, 0.f), rstd = __builtin_amdgcn_rsqf(var + 1e-5f);
        mean = shfl(mean, m * 4); rstd = shfl(rstd, m * 4);
        st8<AUX_SC1>(rws, v2u{f2u(mean), f2u(rstd)}, g ? OOB : OFF_XST + (128 * p + 16 * w + m) * 8, 0);
        WAITV(0);
        BAR();
        gflag_set(rws, (tid == 0 ? b * 256u : OOB) + OFF_QFLAG, get_tag());
        if (tid < 128) *(LAS unsigned*)(lds + L_MISC + tid * 4) = tid < 4 ? 0x3f803f80u : 0u;
        if (hf) {
#pragma unroll
            for (int i = 2; i < 15; ++i) side1(i);
        }
        __builtin_amdgcn_s_sleep(32);
    } else {
        const int wm = w >> 2, wn = w & 3;
        f32x4 acc[4][2] = {}; f32x4 csum = {};
        const bool isv = ct >= 10;
        SBAR();
#pragma unroll
        for (int c = 1; c < 3; ++c) {
            sw += 64 * 3840; if (sw >= 640 * 3840) sw -= 640 * 3840;
            set_m0(ldsa(lds) + w * 4096 + c * K1_SLOT); dmao<0, 2048>(rw, vw0, sw); dmao<0, 3072>(rw, vw1, sw);
        }
            WAITV(4);
            BAR();
        if (isv) qkv_loop<true>(lds, rx, rw, w, va0, va1, vw0, vw1, sx, sw, at, ab, onesv, acc, csum, side1);
        else qkv_loop<false>(lds, rx, rw, w, va0, va1, vw0, vw1, sx, sw, at, ab, onesv, acc, csum, side1);
        if (tid < 128) *(LAS unsigned*)(lds + L_MISC + tid * 4) = tid < 4 ? 0x3f803f80u : 0u;
        if (m == 0) *(LAS f32x4*)(cst + 32 * wn + 16 * wm + 4 * g) = csum;
        if (w == 7) WAITV(4);
        BAR();
        if (!isv) {
            const float sc = ct < 5 ? 0.22360679775f * 1.44269504089f : 1.f;
            f32x4 cs4[2]; f32x2 st[4];
#pragma unroll
            for (int nt = 0; nt < 2; ++nt) cs4[nt] = *(LAS f32x4*)(cst + 32 * wn + 16 * (nt ^ wm) + 4 * g);
#pragma unroll
            for (int mt = 0; mt < 4; ++mt) st[mt] = rst[64 * wm + 16 * (mt ^ wn) + m];
#pragma unroll
            for (int mt = 0; mt < 4; ++mt) {
                const float r = st[mt].y * sc;
#pragma unroll
                for (int nt = 0; nt < 2; ++nt) {
                    f32x4 a = (acc[mt][nt] - st[mt].x * cs4[nt]) * r;
                    st8<AUX_SC1>(rws, v2u{pk2(a[0], a[1]), pk2(a[2], a[3])}, ea[nt][mt & 1], ((mt >> 1) ^ (wn >> 1)) * 1280);
                }
            }
        } else {
            f32x4 mn[4], rs[4]; float cs1[2];
#pragma unroll
            for (int mt = 0; mt < 4; ++mt) {
                f32x4 lo = *(LAS f32x4*)(rst + 64 * wm + 16 * (mt ^ wn) + 4 * g), hi = *(LAS f32x4*)(rst + 64 * wm + 16 * (mt ^ wn) + 4 * g + 2);
                mn[mt] = f32x4{lo[0], lo[2], hi[0], hi[2]}; rs[mt] = f32x4{lo[1], lo[3], hi[1], hi[3]};
            }
#pragma unroll
            for (int nt = 0; nt < 2; ++nt) cs1[nt] = cst[32 * wn + 16 * (nt ^ wm) + m];
#pragma unroll
            for (int nt = 0; nt < 2; ++nt)
#pragma unroll
                for (int u = 0; u < 2; ++u) {
                    f32x4 a = (acc[2 * u][nt] - mn[2 * u] * cs1[nt]) * rs[2 * u], c = (acc[2 * u + 1][nt] - mn[2 * u + 1] * cs1[nt]) * rs[2 * u + 1];
                    unsigned a0 = pk2(a[0], a[1]), a1 = pk2(a[2], a[3]), b0 = pk2(c[0], c[1]), b1 = pk2(c[2], c[3]);
                    v4u o = (wn & 1) ? v4u{b0, b1, a0, a1} : v4u{a0, a1, b0, b1};
                    st16<AUX_SC1>(rws, o, ea[nt][u], 0);
                }
        }
    }
    const unsigned tag = __builtin_amdgcn_readfirstlane(get_tag());
    WAITV(0);
    BAR();
    gflag_set(rws, (tid == 0 ? b * 256u : OOB) + OFF_KFLAG, tag);
    bf16x8 rf0[4], rf1[4];
    retile_read(rtp, rf0); retile_read(rtp + 16384, rf1);
#pragma unroll
    for (int j = 0; j < 4; ++j) { PIN(rf0[j]); PIN(rf1[j]); }
    if (w == 7) {
        const int sec = (lane >> 3) % 3, pp = 2 * Pa - 2 + ((lane >> 1) & 3), c5 = (20 * hd_ + (lane & 1) * 19) >> 7;
        const int pq = (pp < 0 || (sec == 0 && pp < 2 * Pa)) ? 2 * Pa : pp, cq = sec * 5 + c5;
        const int bq = (((pq & 3) * 8 + (cq & 7)) << 3) + (pq >> 2) * 2 + (cq >> 3);
        __builtin_amdgcn_s_sleep(8);
        gflag_wait(rws, OFF_KFLAG + bq * 256, tag);
    }
    BAR();
    {
        const bool skip = Pa == 0 && v < 2;
        const unsigned so = skip ? 0u : (hf ? OFF_V : OFF_K) + hd_ * (64 * 1280) + (8 * Pa - 8) * 1280 + v * 5120;
        const unsigned vo = skip ? OOB : l16;
        set_m0(ldsa(lds) + L_K + w * 5120);
        dmao<AUX_SC1, 0>(rws, vo, so); dmao<AUX_SC1, 1024>(rws, vo, so); dmao<AUX_SC1, 2048>(rws, vo, so); dmao<AUX_SC1, 3072>(rws, vo, so);
        set_m0(ldsa(lds) + L_K + w * 5120 + 4096);
        dmao<AUX_SC1, 0>(rws, vo, so + 4096);
    }
    const unsigned qo = OFF_Q + hd_ * (64 * 1280) + gt * 1280;
    v2u q0a = ld8<AUX_SC1>(rws, hh * 512 + l32 * 8, qo), q0b = ld8<AUX_SC1>(rws, hh * 512 + 256 + l32 * 8, qo);
    v2u q1 = ld8<AUX_SC1>(rws, hh ? OOB : 1024 + l32 * 8, qo);
    SBAR();
    const unsigned wgo = OFF_WGU + cg * (120 * 1024);
    auto marks_late = [&]() {
        int ln = lane; PIN(ln);
        const int g_ = ln >> 4, m_ = ln & 15;
        M.a[2] = OFF_H + ((2 * wt + om) * 80 + 10 * cs) * 256 + op * 1024 + ln * 16;
        M.a[3] = (op || (g_ & 1)) ? OOB : OFF_H + ((2 * wt + om) * 80 + 10 * cs + 8) * 256 + (g_ >> 1) * 256 + m_ * 16;
        M.a[4] = g_ ? OOB : OFF_HST + ((2 * wt + om) * 16 + 2 * cs + op) * 128 + m_ * 8;
        M.a[5] = OFF_A + (2 * ut * 192 + 6 * cg) * 256 + ln * 16;
        M.a[6] = M.a[5] + 192 * 256;
        M.a[7] = OFF_A + ((2 * ut + (g_ & 1)) * 192 + 6 * cg + 4 + (g_ >> 1)) * 256 + m_ * 16;
    };
    unsigned efo, efg;
    {
        const int c0 = (48 * cg + (lane & 1) * 47) >> 6, d0 = (80 * cs + (lane & 1) * 79) >> 6, hq = 8 * v + (lane & 7), mh = m >> 1;
        const int bw = (5 * v + (mh < 5 ? mh : 0)) * 10 + d0, bd = ((6 * w + (mh < 6 ? mh : 0)) * 10 + d0) & 255;
        const int bo = ((4 * P + (hq & 3)) << 3) + (hq >> 2);
        efo = hf ? OFF_KFLAG + (g == 0 ? bw : bo) * 256 : OFF_EFLAG + bd * 256;
        const int bh = (((Pg & 1) * 16 + 4 * v + (lane & 3)) << 3) + (Pg >> 1) * 2 + ((lane >> 2) & 1);
        efg = OFF_EFLAG + (lane < 40 ? ((lane >> 1) * 24 + c0) & 255 : (lane < 48 ? bh : bd)) * 256;
    }
    v4u csl[5];
    bool gok;
    {
        f32x16 mdiag;
#pragma unroll
        for (int r = 0; r < 16; ++r) {
            int i = (r & 3) + 8 * (r >> 2) + 4 * hh, key = (i & 7) | ((i & 8) << 1) | ((i & 16) >> 1);
            mdiag[r] = key <= l32 ? 0.f : -1e30f;
        }
        PIN(mdiag); PIN(efo); PIN(efg);
        PIN(q0a); PIN(q0b); PIN(q1);
        SBAR();
        BAR();
        if (hf == 0) PRIO(2);
        else {
            gflag_wait(rws, efo, tag);
#pragma unroll
            for (int nt = 0; nt < 5; ++nt) {
                const unsigned so = OFF_WO + ((5 * cs + nt) * 20 + 5 * v) * 1024;
                set_m0(ldsa(lds) + (v * 5 + nt) * 5120);
                dmao<AUX_SC1, 0>(rws, l16, so); dmao<AUX_SC1, 1024>(rws, l16, so); dmao<AUX_SC1, 2048>(rws, l16, so); dmao<AUX_SC1, 3072>(rws, l16, so);
                set_m0(ldsa(lds) + (v * 5 + nt) * 5120 + 4096);
                dmao<AUX_SC1, 0>(rws, l16, so + 4096);
            }
            marks_late();
            Marks Ma;
            Ma.a[2] = M.a[2] - 8 * 20480; Ma.a[3] = M.a[3] - 8 * 20480; Ma.a[4] = M.a[4] - 8 * 2048;
#pragma unroll
            for (int i = 2; i < 5; ++i) { mark_store(rws, Ma, i); mark_store(rws, M, i); }
            if (w < 6) retile_store(v + 4, rtt1, lane, rf1, rws);
            retile_store(v, rtt0, lane, rf0, rws);
        }
        SBAR();
        const bf16x8 qa = asb(v4u{q0a[0], q0a[1], q0b[0], q0b[1]}); const s16x4 qb = __builtin_bit_cast(s16x4, q1);
        f32x16 o;
        v2u gf;
        auto side = [&](int pass, int t) {
            if (pass == 0 && t == 3 && hf) { if (w < 6) WAITV(9); else WAITV(5); lf_set(lds, F_E, w); }
            if (pass == 1 && t == 0 && hf) { WAITV(0); lf_set(lds, F_Q, w); }
            if (pass == 1 && t == 1 && w == 7) { lf_wait4(lds, F_Q, 1); gflag_set(rws, (lane == 0 ? b * 256u : OOB) + OFF_EFLAG, tag); }
            if (pass == 1 && t == 8) gf = gflag_load(rws, hf ? efg : OOB);
            SBAR();
        };
        if (Pa == 0) attention<true>(lds, w, lane, mdiag, qa, qb, o, side); else attention<false>(lds, w, lane, mdiag, qa, qb, o, side);
        PRIO(3);
        gok = !gflag_bad(gf, tag);
        auto rr = __builtin_amdgcn_permlane32_swap(f2u(o[8]), f2u(o[8]), false, false);
        float inv = __builtin_amdgcn_rcpf(u2f(rr[1]));
        v4u o16 = {pk2s(o[0] * inv, o[1] * inv), pk2s(o[2] * inv, o[3] * inv), pk2s(o[4] * inv, o[5] * inv), pk2s(o[6] * inv, o[7] * inv)};
        st16<AUX_SC1>(rws, o16, M.a[0], 0);
        v2u o8 = {pk2s(o[8] * inv, o[9] * inv), pk2s(o[10] * inv, o[11] * inv)};
        st8<AUX_SC1>(rws, o8, M.a[1], 0);
        SBAR();
    }
    lf_set(lds, F_ATT, w);
    const unsigned xo = (32 * wt + 16 * om + m) * 1280 + 160 * cs;
    unsigned hp[6];
    v4u wd_r[6][5];
    const unsigned wdo = OFF_WD + (5 * cs * 48 + 6 * w) * 1024;
    auto load_wd = [&](int f) { const int nl = f / 6, na = nl < 4 ? nl ^ (2 * op) : 4, kd = f % 6; wd_r[kd][nl] = ld16<AUX_SC1>(rws, l16 + (kd & 3) * 1024, wdo + (na * 48 + (kd & 4)) * 1024); };
    constexpr int WD_EARLY = 16;
    marks_late();
    if (hf == 0) {
        rsrc_t rxa = mkrs(x, S * D * 2);
                v4u xr[2]; v2u xr4[2];
#pragma unroll
        for (int h2 = 0; h2 < 2; ++h2) { xr[h2] = ld16<0>(rxa, xo + g * 16, op * 64 + h2 * (128 * 1280)); xr4[h2] = ld8<0>(rxa, xo + 128 + g * 8, h2 * (128 * 1280)); }
        SBAR();
        v4u wo_r[5][5];
        lf_wait4(lds, F_E, 1);
        {
            lptr wp0 = lds + (v * 5 + 2 * op) * 5120 + l16, wp1 = lds + (v * 5 + 2 * (op ^ 1)) * 5120 + l16, wp4 = lds + (v * 5 + 4) * 5120 + l16;
#pragma unroll
            for (int nl = 0; nl < 5; ++nl)
#pragma unroll
                for (int k5 = 0; k5 < 5; ++k5) { wo_r[k5][nl] = *(LAS v4u*)((nl < 2 ? wp0 : (nl < 4 ? wp1 : wp4)) + (nl < 4 ? (nl & 1) * 5120 : 0) + k5 * 1024); }
            SBAR();
        }
        lf_set(lds, F_WG0, w);
        lf_wait4(lds, F_WG0, 0);
        v4u af[2][5];
        auto load_o = [&](int h2) {
#pragma unroll
            for (int ml = 1; ml >= 0; --ml) {
                const unsigned so = OFF_O + ((2 * (wt + 4 * h2) + (ml ^ om)) * 20 + 5 * v) * 1024;
#pragma unroll
                for (int ks = 0; ks < 4; ++ks) af[ml][ks] = ld16<AUX_SC1>(rws, l16 + ks * 1024, so);
                v2u lo = ld8<AUX_SC1>(rws, g * 256 + m * 8, so + 4096), hi = ld8<AUX_SC1>(rws, g * 256 + 128 + m * 8, so + 4096);
                af[ml][4] = v4u{lo[0], lo[1], hi[0], hi[1]};
            }
            SBAR();
        };
        const unsigned oc = OFF_O + (2 * wt + 1) * 20480 + v * 5120 + 4096 + (lane & 7) * 128;
        unsigned spins = 0;
        PRIO(0);
        canary_wait(rws, oc, spins);
        load_o(0);
#pragma unroll
        for (int h2 = 0; h2 < 2; ++h2) {
            if (h2) lf_wait4(lds, F_ATT, 1);
            if (h2) load_o(1);
            lptr sc = lds + (h2 ? L_SB : L_SA);
            f32x4 acc[2][5];
#pragma unroll 1
            for (;;) {
                PRIO(3);
                unsigned mx = 0;
#pragma unroll
                for (int ml = 1; ml >= 0; --ml) {
#pragma unroll
                    for (int ks = 0; ks < 5; ++ks) {
                        mx = umax4(mx, af[ml][ks]);
#pragma unroll
                        for (int nl = 4; nl >= 0; --nl) acc[ml][nl] = MFMA16(asb(wo_r[ks][nl]), asb(af[ml][ks]), ks ? acc[ml][nl] : f32x4{});
                        if (ml == 0) *(LAS f32x4*)(sc + v * 8192 + (3 + ks) * 1024 + l16) = acc[1][ks];
                        SGB(0x8, 1); SGB(0x2, 1); SGB(0x8, 1); SGB(0x2, 1); SGB(0x8, 1); SGB(0x200, 1); SGB(0x8, 2);
                        SBAR();
                        PIN(mx);
                    }
                }
#pragma unroll
                for (int nl = 4; nl >= 2; --nl) *(LAS f32x4*)(sc + v * 8192 + (nl - 2) * 1024 + l16) = acc[0][nl];
                if (!any_sent(mx) || ++spins > SPIN_MAX) break;
                PRIO(0);
                canary_wait(rws, oc + h2 * (8 * 20480), spins);
                load_o(h2);
            }
            lf_set(lds, F_WO, 4 * h2 + v);
            lf_wait4(lds, F_WO, h2);
            f32x4 a0 = acc[0][0], a1 = acc[0][1], a4 = acc[0][4];
#pragma unroll
            for (int d = 1; d < 4; ++d) {
                lptr sp = sc + (v ^ d) * 8192 + l16;
                a0 += *(LAS f32x4*)(sp + ((d & 1) * 5 + (0 ^ (d & 2)) - 2) * 1024);
                a1 += *(LAS f32x4*)(sp + ((d & 1) * 5 + (1 ^ (d & 2)) - 2) * 1024);
                a4 += *(LAS f32x4*)(sp + ((d & 1) * 5 + 4 - 2) * 1024);
            }
            unsigned hq[6];
            hq[0] = pk2s(a0[0] + bflo(xr[h2][0]), a0[1] + bfhi(xr[h2][0])); hq[1] = pk2s(a0[2] + bflo(xr[h2][1]), a0[3] + bfhi(xr[h2][1]));
            hq[2] = pk2s(a1[0] + bflo(xr[h2][2]), a1[1] + bfhi(xr[h2][2])); hq[3] = pk2s(a1[2] + bflo(xr[h2][3]), a1[3] + bfhi(xr[h2][3]));
            hq[4] = pk2s(a4[0] + bflo(xr4[h2][0]), a4[1] + bfhi(xr4[h2][0])); hq[5] = pk2s(a4[2] + bflo(xr4[h2][1]), a4[3] + bfhi(xr4[h2][1]));
            st16<AUX_SC1>(rws, v4u{hq[0], hq[1], hq[2], hq[3]}, M.a[2], h2 * (8 * 20480));
            {
                auto r4 = __builtin_amdgcn_permlane16_swap(hq[4], hq[4], false, false);
                auto r5 = __builtin_amdgcn_permlane16_swap(hq[5], hq[5], false, false);
                st16<AUX_SC1>(rws, v4u{r4[0], r5[0], r4[1], r5[1]}, M.a[3], h2 * (8 * 20480));
            }
            float s1 = 0.f, s2 = 0.f;
#pragma unroll
            for (int e = 0; e < 6; ++e) {
                const unsigned hv = (e >= 4 && op) ? 0u : hq[e];
                const float h0 = bflo(hv), h1 = bfhi(hv);
                s1 += h0; s2 = __builtin_fmaf(h0, h0, s2); s1 += h1; s2 = __builtin_fmaf(h1, h1, s2);
            }
            s1 = xor32_add(xor16_add(s1)); s2 = xor32_add(xor16_add(s2));
            unsigned u1 = f2u(s1), u2 = f2u(s2);
            st8<AUX_SC1>(rws, v2u{u1 < 0xfffffffeu ? u1 : 0xfffffffeu, u2 < 0xfffffffeu ? u2 : 0xfffffffeu}, M.a[4], h2 * (8 * 2048));
            if (h2 == 0) {
#pragma unroll
                for (int e = 0; e < 6; ++e) hp[e] = hq[e];
            } else {
                *(LAS v4u*)(lds + L_HP + v * 1536 + l16) = v4u{hq[0], hq[1], hq[2], hq[3]};
                *(LAS v2u*)(lds + L_HP + v * 1536 + 1024 + lane * 8) = v2u{hq[4], hq[5]};
            }
            lf_set(lds, F_WO2, 4 * h2 + v);
        }
        PRIO(0);
        gflag_wait(rws, efo, tag);
#pragma unroll
        for (int f = 0; f < WD_EARLY; f += 2) { load_wd(f); load_wd(f + 1); SBAR(); WAITV(4); }
    } else {
        PRIO(0);
        if (!gok) gflag_wait(rws, efg, tag);
        if (v < 2) {
            const unsigned co = m < 12 ? OFF_CS + (v * 20 + 5 * g) * 6144 + cg * 192 + m * 16 : OOB;
#pragma unroll
            for (int i = 0; i < 5; ++i) csl[i] = ld16<AUX_SC1>(rws, co, i * 6144);
        }
        SBAR();
        lf_wait4(lds, F_WG0, 0);
#pragma unroll
        for (int i = 0; i < 5; ++i) {
            set_m0(ldsa(lds) + (i * 4 + v) * 4096);
            const unsigned so = wgo + (i * 4 + v) * 4096;
            dmao<AUX_SC1, 0>(rws, l16, so); dmao<AUX_SC1, 1024>(rws, l16, so); dmao<AUX_SC1, 2048>(rws, l16, so); dmao<AUX_SC1, 3072>(rws, l16, so);
        }
        if (v < 2) {
            f32x4 cs5 = {};
#pragma unroll
            for (int i = 0; i < 5; ++i) cs5 += __builtin_bit_cast(f32x4, csl[i]);
#pragma unroll
            for (int e = 0; e < 4; ++e) cs5[e] = xor32_add(xor16_add(cs5[e]));
            if (lane < 12) {
                const int t = m < 8 ? (m & 1) : 2, q = m < 8 ? (m >> 1) : (m & 3);
                *(LAS f32x4*)(lds + L_CSUM + (2 * t + v) * 64 + q * 16) = cs5;
            }
        }
        WAITV(0);
        lf_set(lds, F_CS, w);
        lf_wait4(lds, F_WO2, 0);
#pragma unroll
        for (int i = 0; i < 5; ++i) {
            set_m0(ldsa(lds) + 81920 + v * 10240 + i * 2048);
            const unsigned so = wgo + 81920 + v * 10240 + i * 2048;
            dmao<AUX_SC1, 0>(rws, l16, so); dmao<AUX_SC1, 1024>(rws, l16, so);
        }
        PRIO(1);
    }
    lf_wait4(lds, F_CS, 1);
    v4u afd[2][6];
    v2u aflg;
    const unsigned afo = OFF_AFLAG + ((8 * P + rq) * 32 + 4 * w + (lane & 3)) * 256;
    auto load_act = [&](int h2) {
#pragma unroll
        for (int mt = 1; mt >= 0; --mt)
#pragma unroll
            for (int ks = 0; ks < 6; ++ks) afd[mt][ks] = ld16<AUX_SC1>(rws, l16 + (ks & 3) * 1024, OFF_A + ((2 * (8 * P + 4 * h2 + rq) + (mt ^ om)) * 48 + 6 * w + (ks & 4)) * 1024);
        SBAR();
    };
    {
        constexpr int RING = 4, PRE = 4;
        v4u af[RING][2];
        v2u sp[2][4];
        float nmean[2], var[2];
        f32x4 acc[2][6];
        const unsigned hsrc = OFF_H + 2 * ut * 20 * 1024;
        lptr wl = lds + l16, wh = lds + 61440 + l16;
        unsigned spins = 0;
#pragma unroll 1
        for (;;) {
            unsigned mx;
            f32x4 cq[6];
#pragma unroll
            for (int t = 0; t < 6; ++t) {
                cq[t] = *(LAS f32x4*)(lds + L_CSUM + t * 64 + g * 16);
            }
#pragma unroll 1
            for (;;) {
                if (spins) canary_wait(rws, OFF_HST + 2 * ut * 2048 + l32 * 128, spins);
#pragma unroll
                for (int i = 0; i < 2; ++i)
#pragma unroll
                    for (int j = 0; j < 4; ++j) sp[i][j] = ld8<AUX_SC1>(rws, lane * 8 + (i * 4 + j) * 512, OFF_HST + 2 * ut * 2048);
#pragma unroll
                for (int ks = 0; ks < PRE; ++ks)
#pragma unroll
                    for (int i = 0; i < 2; ++i) af[ks][i] = ld16<AUX_SC1>(rws, l16 + (ks & 3) * 1024, hsrc + (i * 20 + (ks & ~3)) * 1024);
                SBAR();
                mx = 0;
#pragma unroll
                for (int i = 0; i < 2; ++i)
#pragma unroll
                    for (int j = 0; j < 4; ++j) mx = umax3(mx, sp[i][j][0], sp[i][j][1]);
                PIN(mx);
                lf_set(lds, F_WG, w);
                if (!any_sent(mx) || ++spins > SPIN_MAX) break;
            }
#pragma unroll
            for (int i = 0; i < 2; ++i) {
                f32x2 ss = (__builtin_bit_cast(f32x2, sp[i][0]) + __builtin_bit_cast(f32x2, sp[i][1])) + (__builtin_bit_cast(f32x2, sp[i][2]) + __builtin_bit_cast(f32x2, sp[i][3]));
                float s1 = xor32_add(xor16_add(ss[0])), s2 = xor32_add(xor16_add(ss[1]));
                nmean[i] = s1 * (-1.f / D); var[i] = __builtin_fmaxf(s2 * (1.f / D) - nmean[i] * nmean[i], 0.f) + 1e-5f;
                PIN(nmean[i]); PIN(var[i]);
            }
            bf16x8 wf[2][6];
#pragma unroll
            for (int t = 0; t < 6; ++t) wf[0][t] = lds16(wl + t * 1024);
#pragma unroll
            for (int i = 0; i < 2; ++i)
#pragma unroll
                for (int t = 0; t < 6; ++t) acc[i][t] = cq[t] * nmean[i];
            SBAR();
#pragma unroll
            for (int ks = 0; ks < 20; ++ks) {
                if (ks == 12) lf_wait4(lds, F_WG, 1);
                if (ks + 1 < 20) {
#pragma unroll
                    for (int t = 0; t < 6; ++t) wf[(ks + 1) & 1][t] = lds16(ks + 1 < 10 ? wl + ((ks + 1) * 6 + t) * 1024 : wh + ((ks - 9) * 6 + t) * 1024);
                }
                mx = umax4(umax4(mx, af[ks % RING][0]), af[ks % RING][1]);
#pragma unroll
                for (int t = 0; t < 6; ++t)
#pragma unroll
                    for (int i = 0; i < 2; ++i) acc[i][t] = MFMA16(wf[ks & 1][t], asb(af[ks % RING][i]), acc[i][t]);
#pragma unroll
                for (int t = 0; t < 6; ++t) { SGB(0x8, 1); SGB(0x100, 1); SGB(0x8, 1); SGB(0x2, 1); }
                SBAR();
                PIN(mx);
                {
                    constexpr int FILL = RING - PRE;
                    const int k0 = ks < FILL ? PRE + 2 * ks : ks + RING, kn = ks < FILL ? 2 : 1;
#pragma unroll
                    for (int q = 0; q < 2; ++q) {
                        const int kk = k0 + q;
                        if (q < kn && kk < 20) {
#pragma unroll
                            for (int i = 0; i < 2; ++i) af[kk % RING][i] = ld16<AUX_SC1>(rws, l16 + (kk & 3) * 1024, hsrc + (i * 20 + (kk & ~3)) * 1024);
                        }
                    }
                }
                SBAR();
            }
            if (!any_sent(mx) || ++spins > SPIN_MAX) break;
        }
        lf_set(lds, F_GU, w);
        PRIO(2);
#pragma unroll
        for (int f = WD_EARLY; f < 30; ++f) load_wd(f);
        SBAR();
        unsigned d[2][6];
#pragma unroll
        for (int i = 0; i < 2; ++i) {
            float r = __builtin_amdgcn_rsqf(var[i]);
            const float k1 = -2.302208198f * r, k3 = -0.1029432397f * r * r * r;
            const f32x2 k1v = {k1, k1}, k3v = {k3, k3}, vv = {var[i], var[i]};
#pragma unroll
            for (int t = 0; t < 3; ++t) {
#pragma unroll
                for (int e = 0; e < 4; e += 2) {
                    f32x2 ga = {acc[i][2 * t][e], acc[i][2 * t][e + 1]}, ua = {acc[i][2 * t + 1][e], acc[i][2 * t + 1][e + 1]};
                    f32x2 gv = ga, uv = ua;
                    f32x2 z = gv * __builtin_elementwise_fma(gv * gv, k3v, k1v);
                    f32x2 ex = {__builtin_amdgcn_exp2f(z[0]), __builtin_amdgcn_exp2f(z[1])};
                    f32x2 dn = __builtin_elementwise_fma(ex, vv, vv);
                    f32x2 rc = {__builtin_amdgcn_rcpf(dn[0]), __builtin_amdgcn_rcpf(dn[1])};
                    f32x2 o = (gv * rc) * uv;
                    d[i][2 * t + (e >> 1)] = pk2(o[0], o[1]);
                }
            }
            PIN(d[i][4]); PIN(d[i][5]);
            SBAR();
            if (i == 1) aflg = gflag_load(rws, hf ? OOB : afo);
            st16<AUX_SC1>(rws, v4u{d[i][0], d[i][1], d[i][2], d[i][3]}, M.a[5 + i], 0);
        }
        auto r4 = __builtin_amdgcn_permlane16_swap(d[0][4], d[1][4], false, false);
        auto r5 = __builtin_amdgcn_permlane16_swap(d[0][5], d[1][5], false, false);
        st16<AUX_SC1>(rws, v4u{r4[0], r5[0], r4[1], r5[1]}, M.a[7], 0);
        WAITV(0);
        gflag_set(rws, lane == 0 ? OFF_AFLAG + (ut * 32 + cg) * 256 : OOB, tag);
        SBAR();
        if (hf) {
#pragma unroll
            for (int f = 0; f < WD_EARLY; f += 2) { load_wd(f); load_wd(f + 1); SBAR(); WAITV(6); }
        }
        SBAR();
    }
    if (b == 0 && w == 4) {
#pragma unroll 1
        for (int i = 0; i < 4; ++i) gflag_wait(rws, OFF_KFLAG + (i * 64 + lane) * 256, tag);
        __builtin_amdgcn_raw_buffer_store_b32(tag, rws, lane == 0 ? OFF_EPOCH : OOB, 0, AUX_SC1);
    }
    {
        if (hf) {
            PRIO(0);
            lf_wait4(lds, F_WO2, 1);
            v4u h4 = *(LAS v4u*)(lds + L_HP + v * 1536 + l16); v2u h2v = *(LAS v2u*)(lds + L_HP + v * 1536 + 1024 + lane * 8);
            hp[0] = h4[0]; hp[1] = h4[1]; hp[2] = h4[2]; hp[3] = h4[3]; hp[4] = h2v[0]; hp[5] = h2v[1];
            lf_set(lds, F_ACT, w);
        }
        if (gflag_bad(aflg, tag)) gflag_wait(rws, afo, tag);
        load_act(0);
        lf_wait8(lds, F_GU);
        f32x4 keep[3];
#pragma unroll
        for (int h2 = 0; h2 < 2; ++h2) {
            f32x4 acc[2][5] = {};
#pragma unroll
            for (int mt = 1; mt >= 0; --mt) {
#pragma unroll
                for (int ks = 0; ks < 6; ++ks)
#pragma unroll
                    for (int nt = 4; nt >= 0; --nt) acc[mt][nt] = MFMA16(asb(wd_r[ks][nt]), asb(afd[mt][ks]), acc[mt][nt]);
                SBAR();
                if (h2 == 0 && mt == 1) aflg = gflag_load(rws, hf ? OOB : afo + 4 * 32 * 256);
                if (h2 == 0 && mt == 0) {
                    if (gflag_bad(aflg, tag)) gflag_wait(rws, afo + 4 * 32 * 256, tag);
                    load_act(1);
                    lf_wait4(lds, F_ACT, 1);
                }
#pragma unroll
                for (int nt = 4; nt >= 0; --nt) if (mt * 5 + nt >= 2 || h2 != hf) *(LAS f32x4*)(lds + h2 * 81920 + (w * 10 + 9 - (mt * 5 + nt)) * 1024 + l16) = acc[mt][nt];
            }
            lf_set(lds, F_DN + h2, w);
            if (h2 == hf) { keep[0] = acc[0][0]; keep[1] = acc[0][1]; keep[2] = acc[0][4]; }
        }
        lf_wait8(lds, F_DN + hf);
        f32x4 a0 = keep[0], a1 = keep[1], a4 = keep[2];
#pragma unroll
        for (int d = 1; d < 8; ++d) {
            lptr sp = lds + hf * 81920 + (w ^ d) * 10240 + l16;
            a0 += *(LAS f32x4*)(sp + (9 - ((d & 1) * 5 + (0 ^ (d & 2)))) * 1024);
            a1 += *(LAS f32x4*)(sp + (9 - ((d & 1) * 5 + (1 ^ (d & 2)))) * 1024);
            a4 += *(LAS f32x4*)(sp + (9 - ((d & 1) * 5 + 4)) * 1024);
        }
        rsrc_t ro = mkrs(out, S * D * 2);
        v4u o = {pk2(a0[0] + bflo(hp[0]), a0[1] + bfhi(hp[0])), pk2(a0[2] + bflo(hp[1]), a0[3] + bfhi(hp[1])),
                 pk2(a1[0] + bflo(hp[2]), a1[1] + bfhi(hp[2])), pk2(a1[2] + bflo(hp[3]), a1[3] + bfhi(hp[3]))};
        st16<0>(ro, o, xo + g * 16, op * 64);
        v2u o4 = {pk2(a4[0] + bflo(hp[4]), a4[1] + bfhi(hp[4])), pk2(a4[2] + bflo(hp[5]), a4[3] + bfhi(hp[5]))};
        st8<0>(ro, o4, op ? OOB : xo + 128 + g * 8, 0);
    }
    asm volatile(".Lmega_end:");
}

#if defined(__HIP_DEVICE_COMPILE__)
#pragma clang attribute pop
#endif

extern "C" void kernel_launch(void* const* d_in, const int* in_sizes, int n_in, void* d_out, int out_size, void* d_ws, size_t ws_size, hipStream_t stream) {
    (void)hipFuncSetAttribute((const void*)mega, hipFuncAttributeMaxDynamicSharedMemorySize, LDS_BYTES);
    mega<<<256, 512, LDS_BYTES, stream>>>((const char*)d_in[0], (const char*)d_in[1], (const char*)d_in[2], (const char*)d_in[4], (const char*)d_in[5], (const char*)d_in[6], (char*)d_out, (char*)d_ws);
}
```
